```python
import torch

def _softmax(x, axis=-1):
    return torch.softmax(x, dim=axis)
def _gelu(x):
    return torch.nn.functional.gelu(x, approximate="tanh")
def _silu(x):
    return torch.nn.functional.silu(x)
def _sigmoid(x):
    return torch.sigmoid(x)
def _layernorm(x, axis=-1, eps=1e-5):
    m = x.mean(dim=axis, keepdim=True)
    v = x.var(dim=axis, keepdim=True, unbiased=False)
    return (x - m) / torch.sqrt(v + eps)
def _scatter_add(tgt, idx, src, axis=0):
    return tgt.clone().index_add_(axis, idx, src)
def _segment_sum(src, seg, n):
    out = torch.zeros((n,) + src.shape[1:], dtype=src.dtype)
    return out.index_add_(0, seg, src)
def _pad(x, pw):
    flat = tuple(v for pair in reversed(pw) for v in pair)
    return torch.nn.functional.pad(x, flat)

kernel_name = 'gated_mix__mha_block_cached__geglu_ffn__decode'

def setup_inputs():

    _w = (lambda a: (lambda t: t.to(torch.bfloat16) if t.is_floating_point() else t)(torch.as_tensor(a)))
    def _A_setup():
        B, Sq, D = 8, 1, 1024
        H, Sk, Dh = 32, 4096, 128
        return {
            "x": _w((lambda *s: torch.randn(*s, dtype=torch.bfloat16) * 0.05)(B, Sq, D)),
            "k_cache": _w((lambda *s: torch.randn(*s, dtype=torch.bfloat16) * 6.4)(B, H, Sk, Dh)),
            "v_cache": _w((lambda *s: torch.randn(*s, dtype=torch.bfloat16) * 0.4)(B, H, Sk, Dh)),
            "wq": _w((lambda *s: torch.randn(*s, dtype=torch.bfloat16) * 6.4)(D, H * Dh) * (D ** -0.5)),
            "wo": _w((lambda *s: torch.randn(*s, dtype=torch.bfloat16) * 0.4)(H * Dh, D) * ((H * Dh) ** -0.5)),
        }
    _A_in = _A_setup()

    def _B_setup():
        B, S, D, F = 8, 1, 1024, 3584
        return {
            "x": _w((lambda *s: torch.randn(*s, dtype=torch.bfloat16) * 0.05)(B, S, D)),
            "w_gate": _w((lambda *s: torch.randn(*s, dtype=torch.bfloat16) * 0.05)(D, F)),
            "w_up": _w((lambda *s: torch.randn(*s, dtype=torch.bfloat16) * 0.05)(D, F)),
            "w_down": _w((lambda *s: torch.randn(*s, dtype=torch.bfloat16) * 0.05)(F, D)),
        }
    _B_in = _B_setup()

    return {
        "A_x": _A_in["x"],
        "A_k_cache": _A_in["k_cache"],
        "A_v_cache": _A_in["v_cache"],
        "A_wq": _A_in["wq"],
        "A_wo": _A_in["wo"],
        "B_x": _B_in["x"],
        "B_w_gate": _B_in["w_gate"],
        "B_w_up": _B_in["w_up"],
        "B_w_down": _B_in["w_down"],
        "gate_w": _w((lambda *s: torch.randn(*s, dtype=torch.bfloat16) * 0.05)(1024) * 16.0),
    }

def reference(A_x, A_k_cache, A_v_cache, A_wq, A_wo, B_x, B_w_gate, B_w_up, B_w_down, gate_w):
    def _A_ref(x, k_cache, v_cache, wq, wo):
        B, Sq, D = x.shape
        H, Dh = 32, 128
        q = (lambda x, *d: x.permute(*d))((x @ wq).reshape(B, Sq, H, Dh), 0, 2, 1, 3)
        scores = torch.einsum("bhqd,bhkd->bhqk", q, k_cache) * (Dh ** -0.5)
        p = _softmax(scores, axis=-1)
        o = torch.einsum("bhqk,bhkd->bhqd", p, v_cache)
        return (lambda x, *d: x.permute(*d))(o, 0, 2, 1, 3).reshape(B, Sq, H * Dh) @ wo

    def _B_ref(x, w_gate, w_up, w_down):
        return (_gelu(x @ w_gate) * (x @ w_up)) @ w_down

    x = A_x
    g = _sigmoid((x * gate_w).sum(-1, keepdims=True))
    a = _A_ref(x, A_k_cache, A_v_cache, A_wq, A_wo)
    b = _B_ref(x, B_w_gate, B_w_up, B_w_down)
    return g * a + (1 - g) * b
```

```cpp
#include <hip/hip_runtime.h>
#include <cstdint>

typedef unsigned short u16;
typedef unsigned int u32;
typedef unsigned long long u64;
using u32x4 = __attribute__((ext_vector_type(4))) unsigned;
using u32x2 = __attribute__((ext_vector_type(2))) unsigned;
using f32x4 = __attribute__((ext_vector_type(4))) float;
using bf16x2 = __attribute__((ext_vector_type(2))) __bf16;
using bf16x8 = __attribute__((ext_vector_type(8))) __bf16;
using s16x4 = __attribute__((ext_vector_type(4))) short;
using s16x8 = __attribute__((ext_vector_type(8))) short;
#define LAS __attribute__((address_space(3)))
#define GAS __attribute__((address_space(1)))
typedef __amdgpu_buffer_rsrc_t rsrc_t;

constexpr int NB = 8, DM = 1024, NH = 32, SK = 4096, DH = 128, FF = 3584;
constexpr int NSW = 4;
constexpr int NSV = 4;
constexpr int NWAVES = NSW + NSV;
constexpr int NTHR = NWAVES * 64;
#define PACE 8
#define RING 4
constexpr int UB = RING;
#define LV_NORM 6
#define LV_WIDE 8
#define LV_NARROW 5
#define LV_PRO LV_NORM
#define CT16 122
#define D_WIDE 40
#define D_NARROW 40
#define CSL2 3
#define SLOT_LG 9
#define DRIFT_MAX 0.08f
#define D_NARROW2 120
#define LV_NARROW2 4
#define POSL 2, 3, 6, 7, 9, 4, 10, 11, 5, 8, 0, 1
#define OGDELAY 12
#define WOTRIG 1
#define APDELAY 40
constexpr int TPW = SK / 4 / NSW;
constexpr int AUX_NT = 2, AUX_SC1 = 16;
constexpr int RSRC3 = 0x00020000;
#define QDELAY 16
#define QSLEEP 4
#define T_GU 1800
#define OGSLEEP 2
#define APSLEEP 2

constexpr int L_WQ = 0;
constexpr int L_FB = L_WQ + 32768;
constexpr int L_WO = L_FB + 65536;
constexpr int L_XS = L_WO + 32768;
constexpr int L_XQ = L_XS + 16384;
constexpr int L_Q = L_XQ + 2048;
constexpr int L_FLG = L_Q + 256;
constexpr int L_ML = L_FLG + 64;
constexpr int L_MRG = L_ML + 128;
constexpr int L_OS = L_MRG + NSW * 4 * 512;
constexpr int ACT_PITCH = 272;
constexpr int L_HS = L_OS + 8 * ACT_PITCH;
constexpr int L_END = L_HS + 8 * ACT_PITCH + 192;
static_assert(L_MRG % 16 == 0 && L_OS % 16 == 0 && L_HS % 16 == 0, "align");
static_assert(L_END <= 163840, "LDS");

constexpr u32 W_E = 0;
constexpr u32 W_QP = 4096;
constexpr u32 W_OG = W_QP + 32 * 8 * 8 * 64 * 16;
constexpr u32 W_AP = W_OG + 32 * 8 * 32 * 16;
constexpr u32 W_GU = W_AP + 8 * 32 * 8 * 64 * 16;
constexpr u32 W_BP = W_GU + 2 * 8 * 896 * 16;
constexpr u32 W_PR = W_BP + 28 * 8 * 512 * 16;
constexpr u32 PR_PITCH = 4096;
constexpr u32 W_END = W_PR + 4 * PR_PITCH + 4096;
#define SPINCAP (1u << 20)
constexpr u32 SPIN_CAP = SPINCAP;

__device__ __forceinline__ float bflo(u32 x) { return __uint_as_float(x << 16); }
__device__ __forceinline__ float bfhi(u32 x) { return __uint_as_float(x & 0xffff0000u); }
__device__ __forceinline__ u32 pk2(float lo, float hi) {
  bf16x2 v = {(__bf16)lo, (__bf16)hi};
  return __builtin_bit_cast(u32, v);
}
__device__ __forceinline__ float rbf(float f) { return __uint_as_float((u32)__builtin_bit_cast(u16, (__bf16)f) << 16); }
__device__ __forceinline__ float dot2(u32 a, u32 b, float acc) {
  return __builtin_amdgcn_fdot2_f32_bf16(__builtin_bit_cast(bf16x2, a), __builtin_bit_cast(bf16x2, b), acc, false);
}
template <int CTRL> __device__ __forceinline__ float dpp(float x) {
  return __builtin_bit_cast(float, __builtin_amdgcn_mov_dpp(__builtin_bit_cast(int, x), CTRL, 0xf, 0xf, true));
}
__device__ __forceinline__ float row_sum16(float d) {
  d += dpp<0x128>(d); d += dpp<0x124>(d); d += dpp<0x122>(d); d += dpp<0x121>(d);
  return d;
}
__device__ __forceinline__ float x16(float x) { return __shfl_xor(x, 16); }
__device__ __forceinline__ float x32(float x) { return __shfl_xor(x, 32); }
__device__ __forceinline__ rsrc_t mk_rsrc(const void* p, u32 bytes) { return __builtin_amdgcn_make_buffer_rsrc((void*)p, 0, bytes, RSRC3); }
__device__ __forceinline__ void dma16(rsrc_t rs, LAS char* dst, u32 voff, u32 soff) {
  __builtin_amdgcn_raw_ptr_buffer_load_lds(rs, (LAS void*)dst, 16, voff, soff, 0, AUX_NT);
}
__device__ __forceinline__ void dma16d(rsrc_t rs, LAS char* dst, u32 voff, u32 soff) {
  __builtin_amdgcn_raw_ptr_buffer_load_lds(rs, (LAS void*)dst, 16, voff, soff, 0, 0);
}
__device__ __forceinline__ u32x4 ldg(rsrc_t rs, u32 off) { return __builtin_amdgcn_raw_buffer_load_b128(rs, off, 0, 0); }
__device__ __forceinline__ u32 swz(u32 row) { return ((row & 3) << 2) | ((row >> 2) & 3); }
__device__ __forceinline__ f32x4 mfma16(s16x8 a, u32x4 b, f32x4 c) {
  return __builtin_amdgcn_mfma_f32_16x16x32_bf16(__builtin_bit_cast(bf16x8, a), __builtin_bit_cast(bf16x8, b), c, 0, 0, 0);
}
__device__ __forceinline__ s16x8 tr_pair(LAS char* p0, LAS char* p1) {
  s16x4 lo = __builtin_amdgcn_ds_read_tr16_b64_v4i16((LAS s16x4*)p0);
  s16x4 hi = __builtin_amdgcn_ds_read_tr16_b64_v4i16((LAS s16x4*)p1);
  return __builtin_shufflevector(lo, hi, 0, 1, 2, 3, 4, 5, 6, 7);
}
__device__ __forceinline__ s16x8 afrag_swz(LAS char* tile, int k0, int ct, int lane) {
  const u32 g = lane >> 4, q = (lane & 15) >> 2, p = lane & 3;
  const u32 r0 = k0 + 8 * g + q, r1 = r0 + 4, ch = 2 * ct + (p >> 1);
  return tr_pair(tile + 256 * r0 + 16 * (ch ^ swz(r0)) + 8 * (p & 1), tile + 256 * r1 + 16 * (ch ^ swz(r1)) + 8 * (p & 1));
}
template <int PITCH> __device__ __forceinline__ s16x8 afrag_lin(LAS char* tile, int k0, int ct, int lane) {
  const u32 g = lane >> 4, q = (lane & 15) >> 2, p = lane & 3;
  LAS char* a = tile + PITCH * (k0 + 8 * g + q) + 32 * ct + 8 * p;
  return tr_pair(a, a + 4 * PITCH);
}
__device__ __forceinline__ u32x4 ldg_sc1(rsrc_t rs, u32 off) { return __builtin_amdgcn_raw_buffer_load_b128(rs, off, 0, AUX_SC1); }
__device__ __forceinline__ void stg_sc1(rsrc_t rs, u32 off, u32 v0, u32 v1, u32 tag) {
  u32x4 v = {v0, tag, v1, ~tag};
  __builtin_amdgcn_raw_buffer_store_b128(v, rs, off, 0, AUX_SC1);
}
__device__ __forceinline__ bool gok(u32x4 g, u32 tag) { return (g[1] == tag) & (g[3] == ~tag); }
template <int N, typename F> __device__ __forceinline__ void gather_issue(u32x4 (&g)[N], rsrc_t rs, F off) {
#pragma unroll
  for (int i = 0; i < N; ++i) g[i] = ldg_sc1(rs, off(i));
}
template <int SLEEP, int N, typename F> __device__ __forceinline__ u32 gather_finish(u32x4 (&g)[N], rsrc_t rs, F off, u32 tag) {
  u32 spin;
  for (spin = 0; spin < SPIN_CAP; ++spin) {
    bool ok = true;
#pragma unroll
    for (int i = 0; i < N; ++i) ok &= gok(g[i], tag);
    if (__all(ok)) break;
    __builtin_amdgcn_s_sleep(SLEEP);
    u32x4 t[N];
#pragma unroll
    for (int i = 0; i < N; ++i) t[i] = ldg_sc1(rs, gok(g[i], tag) ? 0x7ffffff0u : off(i));
#pragma unroll
    for (int i = 0; i < N; ++i) { const bool k = gok(g[i], tag); g[i] = k ? g[i] : t[i]; }
  }
  return spin;
}
__device__ __forceinline__ float sum32(float x) {
  const auto r = __builtin_amdgcn_permlane32_swap(__float_as_uint(x), __float_as_uint(x), false, false);
  return __uint_as_float(r[0]) + __uint_as_float(r[1]);
}
__device__ __forceinline__ float sum16(float x) {
  const auto r = __builtin_amdgcn_permlane16_swap(__float_as_uint(x), __float_as_uint(x), false, false);
  return __uint_as_float(r[0]) + __uint_as_float(r[1]);
}
__device__ __forceinline__ float max32(float x) {
  const auto r = __builtin_amdgcn_permlane32_swap(__float_as_uint(x), __float_as_uint(x), false, false);
  return fmaxf(__uint_as_float(r[0]), __uint_as_float(r[1]));
}
__device__ __forceinline__ void stg_frag(rsrc_t rs, u32 off_first, f32x4 acc, int n16, u32 tag) {
  const bool lo = n16 < 8;
  stg_sc1(rs, off_first + (lo ? 0u : 16u), __float_as_uint(lo ? acc[0] : acc[2]), __float_as_uint(lo ? acc[1] : acc[3]), tag);
}
__device__ __forceinline__ void bar() { __builtin_amdgcn_s_barrier(); }
#define SCHED() __builtin_amdgcn_sched_barrier(0)
#define WAIT_VM(n) asm volatile("s_waitcnt vmcnt(" #n ")" ::: "memory")
#define STR_(x) #x
#define WAIT_VMX(x) asm volatile("s_waitcnt vmcnt(" STR_(x) ")" ::: "memory")
template <int P> __device__ __forceinline__ void wait_vm_lt() {
  if (P == 1) WAIT_VM(0); else if (P == 2) WAIT_VM(1); else if (P == 3) WAIT_VM(2); else if (P == 4) WAIT_VM(3);
  else if (P == 5) WAIT_VM(4); else if (P == 6) WAIT_VM(5); else if (P == 7) WAIT_VM(6); else if (P == 8) WAIT_VM(7);
  else if (P == 11) WAIT_VM(10); else if (P == 9) WAIT_VM(8); else if (P == 10) WAIT_VM(9); else if (P == 12) WAIT_VM(11); else if (P == 16) WAIT_VM(15);
}
#define PACE_WAIT() wait_vm_lt<PACE>()
#define WAIT_LGKM0() asm volatile("s_waitcnt lgkmcnt(0)" ::: "memory")
__device__ __forceinline__ u32 lds_ld(LAS char* a) { return __hip_atomic_load((LAS u32*)a, __ATOMIC_RELAXED, __HIP_MEMORY_SCOPE_WORKGROUP); }
__device__ __forceinline__ void lds_wait(LAS char* a, u32 v) {
  for (u32 spin = 0; spin < SPIN_CAP * 16u; ++spin) {
    if (__builtin_amdgcn_readfirstlane(lds_ld(a)) >= v) break;
    __builtin_amdgcn_s_sleep(1);
  }
  asm volatile("" ::: "memory");
}
__device__ __forceinline__ void lds_arrive(LAS char* a, int lane) {
  WAIT_LGKM0();
  if (lane == 0) __hip_atomic_fetch_add((LAS u32*)a, 1u, __ATOMIC_RELAXED, __HIP_MEMORY_SCOPE_WORKGROUP);
}
__device__ __forceinline__ void wait_ticks(u64 t0, u32 ticks) {
  for (u32 spin = 0; spin < 4096u; ++spin) {
    if (__builtin_amdgcn_s_memrealtime() - t0 >= ticks) break;
    __builtin_amdgcn_s_sleep(16);
  }
}


__device__ __forceinline__ void throttle(u32 lvl) {
  asm volatile(
      "s_waitcnt vmcnt(%4)\n\t"
      "s_cmp_eq_u32 %0, 1\n\t"
      "s_cbranch_scc1 .Lthr%=\n\t"
      "s_waitcnt vmcnt(%1)\n\t"
      "s_cmp_eq_u32 %0, 0\n\t"
      "s_cbranch_scc1 .Lthr%=\n\t"
      "s_waitcnt vmcnt(%2)\n\t"
      "s_cmp_eq_u32 %0, 2\n\t"
      "s_cbranch_scc1 .Lthr%=\n\t"
      "s_waitcnt vmcnt(%3)\n"
      ".Lthr%=:" ::"s"(lvl), "n"(LV_NORM - 1), "n"(LV_NARROW - 1), "n"(LV_NARROW2 - 1), "n"(LV_WIDE - 1) : "scc", "memory");
}
struct Params {
  const u16 *x, *kc, *vc, *wq, *wo, *wg, *wu, *wd, *gw;
  u16* out;
  char* ws;
};

__global__ void __launch_bounds__(NTHR) fused_kernel(Params p) {
  extern __shared__ __attribute__((aligned(16))) char smem_g[];
  LAS char* sm = (LAS char*)smem_g;
  const int tid = threadIdx.x, lane = tid & 63, wave = __builtin_amdgcn_readfirstlane(tid >> 6);
  const int j = blockIdx.x, hh = j & 31, bb = j >> 5;
  const rsrc_t rws = mk_rsrc(p.ws, W_END);
  const int g4 = lane >> 4, n16 = lane & 15;
  u32 tag = 0;
  const int ob = hh >> 2, oc = bb * 128 + (hh & 3) * 32;
  float gate = 0.f;
  const int par = bb & 1, hf = bb < 6 ? (bb >> 1) * 32 + hh : (par ? (hh < 16 ? 96 + hh : 112) : (hh >= 16 ? 80 + hh : 112));
  const bool role1 = hf < 112 && par == 0, role2 = hf < 112 && par == 1;
  const int f2 = hf >> 2, f2c = hf & 3;

  if (wave < NSW) {
    const rsrc_t rk = mk_rsrc(p.kc + (size_t)j * SK * DH, SK * DH * 2);
    const rsrc_t rv = mk_rsrc(p.vc + (size_t)j * SK * DH, SK * DH * 2);
    u32x4 kk[UB], vv[UB];
    constexpr int POS[12] = {POSL};
    const u32 wpart = ((u32)(wave & 1) << POS[8]) | ((u32)(wave >> 1) << POS[9]);
    auto soff_of = [&](int t) -> u32 {
      u32 y = wpart | (((u32)t >> 8) << 12);
#pragma unroll
      for (int i = 0; i < 8; ++i) y |= (((u32)t >> i) & 1u) << POS[i];
      return y * 256u;
    };
    const u32 loff = n16 * 16 + ((((u32)g4 & 1u) << POS[10]) | (((u32)g4 >> 1) << POS[11])) * 256u;
#define LOADK(u, t) kk[u] = __builtin_amdgcn_raw_buffer_load_b128(rk, loff, soff_of(t), AUX_NT)
#define LOADV(u, t) vv[u] = __builtin_amdgcn_raw_buffer_load_b128(rv, loff, soff_of(t), AUX_NT)
    bar();
    SCHED();
#pragma unroll
    for (int u = 0; u < UB; ++u) {
      if (2 * u >= LV_PRO) wait_vm_lt<LV_PRO>();
      SCHED();
      LOADK(u, u);
      SCHED();
      if (2 * u + 1 >= LV_PRO) wait_vm_lt<LV_PRO>();
      SCHED();
      LOADV(u, u);
      SCHED();
    }
    lds_wait(sm + L_FLG + 8, 1u);
    const u32x4 qv = *(LAS u32x4*)(sm + L_Q + n16 * 16);
    float m = -INFINITY, l = 0.f, acc[8];
#pragma unroll
    for (int i = 0; i < 8; ++i) acc[i] = 0.f;
    constexpr float SCALE = 0.08838834764831845f, LOG2E = 1.4426950408889634f;
    auto step = [&](u32x4& kr, u32x4& vr, int tnext, u32 lvl) {
      float d = 0.f;
#pragma unroll
      for (int i = 0; i < 4; ++i) d = dot2(kr[i], qv[i], d);
      SCHED();
      throttle(lvl);
      kr = __builtin_amdgcn_raw_buffer_load_b128(rk, loff, soff_of(tnext), AUX_NT);
      SCHED();
      float s = row_sum16(d) * (SCALE * LOG2E);
      if (__builtin_amdgcn_ballot_w64(s > m) != 0ull) {
        const float mx = fmaxf(m, s);
        const float al = __builtin_amdgcn_exp2f(m - mx);
        m = mx;
        l *= al;
#pragma unroll
        for (int i = 0; i < 8; ++i) acc[i] *= al;
      }
      const float pr = __builtin_amdgcn_exp2f(s - m);
      l += pr;
#pragma unroll
      for (int i = 0; i < 4; ++i) {
        acc[2 * i] = fmaf(pr, bflo(vr[i]), acc[2 * i]);
        acc[2 * i + 1] = fmaf(pr, bfhi(vr[i]), acc[2 * i + 1]);
      }
      SCHED();
      throttle(lvl);
      vr = __builtin_amdgcn_raw_buffer_load_b128(rv, loff, soff_of(tnext), AUX_NT);
      SCHED();
    };
    static_assert(TPW % UB == 0, "ring");
    for (int it = 0; it < TPW; it += UB) {
      u32 lvl = 0;
#pragma unroll
      for (int u = 0; u < UB; ++u) {
        if (u % 4 == 0) {
          lvl = __builtin_amdgcn_readfirstlane(lds_ld(sm + L_FLG + 24));
          if (lane == 0) *(LAS u32*)(sm + L_FLG + 32 + wave * 4) = it + u;
        }
        step(kk[u], vv[u], it + u + UB, lvl);
      }
    }
    {
      LAS float* rec = (LAS float*)(sm + L_MRG + (wave * 4 + g4) * 512 + n16 * 32);
      *(LAS f32x4*)rec = f32x4{acc[0], acc[1], acc[2], acc[3]};
      *(LAS f32x4*)(rec + 4) = f32x4{acc[4], acc[5], acc[6], acc[7]};
      if (n16 == 0) { ((LAS float*)(sm + L_ML))[wave * 4 + g4] = m; ((LAS float*)(sm + L_ML))[16 + wave * 4 + g4] = l; }
      lds_arrive(sm + L_FLG + 28, lane);
    }
  } else {
    __builtin_amdgcn_s_setprio(3);
    const u64 t0 = __builtin_amdgcn_s_memrealtime();
    const int sw = wave - NSW;
    const int fm = hf & 1, fcb = hf >> 1;
    const u32 e_raw = __hip_atomic_load((GAS u32*)(p.ws + W_E), __ATOMIC_RELAXED, __HIP_MEMORY_SCOPE_AGENT);
    const rsrc_t rx = mk_rsrc(p.x, NB * DM * 2);
    if (sw == 0) {
#pragma unroll
      for (int i = 0; i < 2; ++i) { const u32 b = 4 * i + g4; dma16d(rx, sm + L_XQ + i * 1024, b * 2048 + ((n16 ^ (2 * b)) << 4), bb * 256); }
    }
    {
      const rsrc_t rq = mk_rsrc(p.wq, DM * NH * DH * 2);
#pragma unroll
      for (int i = 0; i < 8; ++i) {
        const u32 row = 32 * sw + 4 * i + g4;
        dma16(rq, sm + L_WQ + (8 * sw + i) * 1024, row * 8192 + ((n16 ^ swz(row)) << 4), bb * 128 * 8192 + hh * 256);
      }
    }
    if (sw == 0) { if (lane >= 1 && lane < 16) *(LAS u32*)(sm + L_FLG + lane * 4) = 0u; WAIT_LGKM0(); }
    bar();
    WAIT_VM(0);
    tag = __builtin_amdgcn_readfirstlane(e_raw) + 1u;
    lds_arrive(sm + L_FLG + 4, lane);
    if (sw == 0 && lane == 0) *(LAS u32*)(sm + L_FLG) = tag;
    lds_wait(sm + L_FLG + 4, NSV);
    {
      u32x4 xq[4];
#pragma unroll
      for (int ks = 0; ks < 4; ++ks) xq[ks] = *(LAS u32x4*)(sm + L_XQ + (lane & 7) * 256 + (((4 * ks + g4) ^ (2 * (lane & 7))) << 4));
#pragma unroll
      for (int c = 0; c < 2; ++c) {
        f32x4 acc = {0.f, 0.f, 0.f, 0.f};
#pragma unroll
        for (int ks = 0; ks < 4; ++ks) acc = mfma16(afrag_swz(sm + L_WQ, 32 * ks, 2 * sw + c, lane), xq[ks], acc);
        stg_frag(rws, W_QP + ((((hh * 8 + (lane & 7)) * 8 + bb) * 64) + 8 * (2 * sw + c) + 2 * g4) * 16, acc, n16, tag);
      }
      lds_arrive(sm + L_FLG + 16, lane);
    }
    LAS char* xsq = sm + L_XS + sw * 4096;
    if (role1) {
#pragma unroll
      for (int i = 0; i < 4; ++i) { const u32 b = 2 * i + (lane >> 5); dma16d(rx, xsq + i * 1024, b * 2048 + (((lane & 31) ^ (2 * b)) << 4), sw * 512); }
    }
    if (sw == 0) {
      u32x4 qg[8];
      const u32 qoff = W_QP + (((hh * 8 + bb) * 8) * 64 + lane) * 16;
      auto qo = [&](int i) { return qoff + i * 1024; };
      __builtin_amdgcn_s_sleep(QDELAY);
      gather_issue(qg, rws, qo);
      gather_finish<QSLEEP>(qg, rws, qo, tag);
      float q0 = 0.f, q1 = 0.f;
#pragma unroll
      for (int i = 0; i < 8; ++i) { q0 += __uint_as_float(qg[i][0]); q1 += __uint_as_float(qg[i][2]); }
      *(LAS u32*)(sm + L_Q + lane * 4) = pk2(q0, q1);
      lds_arrive(sm + L_FLG + 8, lane);
    }
    LAS char* fbq = sm + L_FB + sw * 16384;
    LAS char* woq = sm + L_WO + sw * 8192;
    if (role1) {
      const rsrc_t rf = mk_rsrc(fm ? p.wu : p.wg, DM * FF * 2);
      const u32 voff = (lane >> 3) * (FF * 2) + (lane & 7) * 16;
      const u32 sbase = 256 * sw * (FF * 2) + fcb * 128;
      constexpr u32 RS8 = FF * 2;
      LAS char* wqq = sm + L_WQ + sw * 8192;
#pragma unroll
      for (int i = 0; i < 16; ++i) { PACE_WAIT(); dma16(rf, fbq + i * 1024, voff, sbase + i * 8 * RS8); }
#pragma unroll
      for (int i = 0; i < 8; ++i) { PACE_WAIT(); dma16(rf, woq + i * 1024, voff, sbase + (128 + i * 8) * RS8); }
      lds_wait(sm + L_FLG + 16, NSV);
#pragma unroll
      for (int i = 0; i < 8; ++i) { PACE_WAIT(); dma16(rf, wqq + i * 1024, voff, sbase + (192 + i * 8) * RS8); }
      f32x4 acc[4];
#pragma unroll
      for (int t = 0; t < 4; ++t) acc[t] = f32x4{0.f, 0.f, 0.f, 0.f};
      u32x4 xf[8];
      WAIT_VM(8);
#pragma unroll
      for (int ks = 0; ks < 8; ++ks) xf[ks] = *(LAS u32x4*)(xsq + (lane & 7) * 512 + (((4 * ks + g4) ^ (2 * (lane & 7))) << 4));
#pragma unroll
      for (int ks = 0; ks < 4; ++ks)
#pragma unroll
        for (int t = 0; t < 4; ++t) acc[t] = mfma16(afrag_lin<128>(fbq, 32 * ks, t, lane), xf[ks], acc[t]);
      WAIT_VM(8);
#pragma unroll
      for (int ks = 0; ks < 2; ++ks)
#pragma unroll
        for (int t = 0; t < 4; ++t) acc[t] = mfma16(afrag_lin<128>(woq, 32 * ks, t, lane), xf[4 + ks], acc[t]);
      WAIT_LGKM0();
      WAIT_VM(0);
#pragma unroll
      for (int ks = 0; ks < 2; ++ks)
#pragma unroll
        for (int t = 0; t < 4; ++t) acc[t] = mfma16(afrag_lin<128>(wqq, 32 * ks, t, lane), xf[6 + ks], acc[t]);
      if (n16 < 8) {
#pragma unroll
        for (int t = 0; t < 4; ++t) *(LAS f32x4*)(xsq + ((t * 4 + g4) * 8 + n16) * 16) = acc[t];
      }
      lds_arrive(sm + L_FLG + 12, lane);
      lds_wait(sm + L_FLG + 12, NSV);
      if (n16 < 8) {
        f32x4 s = {0.f, 0.f, 0.f, 0.f};
#pragma unroll
        for (int w = 0; w < NSV; ++w) s += *(LAS f32x4*)(sm + L_XS + w * 4096 + ((sw * 4 + g4) * 8 + n16) * 16);
        stg_sc1(rws, W_GU + ((fm * 8 + n16) * 896 + fcb * 16 + 4 * sw + g4) * 16, pk2(s[0], s[1]), pk2(s[2], s[3]), tag);
      }
    } else if (role2) {
      const rsrc_t ro = mk_rsrc(p.wo, NH * DH * DM * 2);
#pragma unroll
      for (int i = 0; i < 8; ++i) {
        const u32 row = 32 * sw + 4 * i + g4;
        PACE_WAIT();
        dma16(ro, woq + i * 1024, row * 2048 + ((n16 ^ swz(row)) << 4), hh * 128 * 2048 + bb * 256);
      }
      wait_ticks(t0, T_GU);
      u32x4 gu[2];
      const u32 goff = W_GU + ((lane >> 3) * 896 + f2 * 32 + sw * 8 + (lane & 7)) * 16;
      auto go = [&](int i) { return goff + i * (8 * 896 * 16); };
      gather_issue(gu, rws, go);
      gather_finish<16>(gu, rws, go, tag);
      float hv[4];
#pragma unroll
      for (int e = 0; e < 4; ++e) {
        const u32 gw_ = gu[0][(e >> 1) * 2], uw_ = gu[1][(e >> 1) * 2];
        const float a = (e & 1) ? bfhi(gw_) : bflo(gw_), u_ = (e & 1) ? bfhi(uw_) : bflo(uw_);
        const float t = 0.7978845608028654f * (a + 0.044715f * a * a * a);
        hv[e] = rbf(0.5f * a * (1.f + tanhf(t))) * u_;
      }
      *(LAS u32x2*)(sm + L_HS + (lane >> 3) * ACT_PITCH + (32 * sw + 4 * (lane & 7)) * 2) = u32x2{pk2(hv[0], hv[1]), pk2(hv[2], hv[3])};
      lds_arrive(sm + L_FLG + 12, lane);
      lds_wait(sm + L_FLG + 12, NSV);
    }
    if (sw == 0) {
      {
        float s = 0.f;
        const rsrc_t rg = mk_rsrc(p.gw, DM * 2);
#pragma unroll
        for (int i = 0; i < 2; ++i) {
          const u32x4 w = ldg(rg, lane * 32 + i * 16);
          const u32x4 xv = ldg(rx, ob * 2048 + lane * 32 + i * 16);
#pragma unroll
          for (int e = 0; e < 4; ++e) { s += rbf(bflo(xv[e]) * bflo(w[e])); s += rbf(bfhi(xv[e]) * bfhi(w[e])); }
        }
        s = row_sum16(s); s += x16(s); s += x32(s);
        s = rbf(s);
        gate = rbf(1.f / (1.f + __expf(-s)));
      }
    }
    if (sw == 3 && role2) { WAIT_VM(0); lds_arrive(sm + L_FLG + 52, lane); }
    if (sw == 3) {
      u32 kprev = 0, tref = 0, base = 0;
      float lref = 0.f, drift = 0.f;
      bool first = true, have = false;
      for (u32 spin = 0; spin < 65536u; ++spin) {
        if (__builtin_amdgcn_readfirstlane(lds_ld(sm + L_FLG + 28)) >= (u32)WOTRIG) break;
        u32 P = 0;
#pragma unroll
        for (int w = 0; w < NSW; ++w) P += lds_ld(sm + L_FLG + 32 + w * 4);
        const u32 t = (u32)__builtin_amdgcn_s_memrealtime();
        const u32 lag = t - ((P * CT16) >> 4);
        const u32 k = t >> SLOT_LG;
        if (first) { base = lag; first = false; kprev = k - 1u; }
        if (k != kprev) {
          kprev = k;
          if (lane == 0) __builtin_amdgcn_raw_buffer_store_b64(u32x2{lag, tag ^ (k * 0x9E3779B9u)}, rws, W_PR + (k & 3u) * PR_PITCH + j * 8, 0, AUX_SC1);
          const u32 roff = W_PR + ((k - 1u) & 3u) * PR_PITCH + lane * 32, rt = tag ^ ((k - 1u) * 0x9E3779B9u);
          const u32x4 e0 = ldg_sc1(rws, roff), e1 = ldg_sc1(rws, roff + 16);
          float sum = 0.f, cnt = 0.f;
          auto add = [&](u32 v, u32 tw) {
            const int d = (int)(v - base);
            const bool ok = tw == rt && d < 8192 && d > -8192;
            sum += ok ? (float)d : 0.f; cnt += ok ? 1.f : 0.f;
          };
          add(e0[0], e0[1]); add(e0[2], e0[3]); add(e1[0], e1[1]); add(e1[2], e1[3]);
          sum = row_sum16(sum); sum += x16(sum); sum += x32(sum);
          cnt = row_sum16(cnt); cnt += x16(cnt); cnt += x32(cnt);
          if (cnt >= 128.f) {
            const float ln = sum / cnt;
            const u32 tn = (k - 1u) << SLOT_LG;
            if (have) drift = fminf(fmaxf((ln - lref) / (float)(int)(tn - tref), -DRIFT_MAX), DRIFT_MAX);
            lref = ln; tref = tn; have = true;
          }
        }
        if (have) {
          const float e = (float)(int)(lag - base) - (lref + drift * (float)(int)(t - tref));
          const u32 lvl = e > (float)D_WIDE ? 1u : (e < (float)-D_NARROW2 ? 3u : (e < (float)-D_NARROW ? 2u : 0u));
          if (lane == 0) *(LAS u32*)(sm + L_FLG + 24) = lvl;
        }
        __builtin_amdgcn_s_sleep(CSL2);
      }
    }
    WAIT_VM(0);
    if (sw != 3 && role2) lds_arrive(sm + L_FLG + 52, lane);
  }
  auto dn_pair = [&](int q0) {
    const rsrc_t rf = mk_rsrc(p.wd, FF * DM * 2);
    const u32 voff = (lane >> 3) * (DM * 2) + (lane & 7) * 16;
    const u32 sbase = f2 * 128 * (DM * 2) + f2c * 512;
#pragma unroll
    for (int q = 0; q < 2; ++q)
#pragma unroll
      for (int i = 0; i < 16; ++i) dma16(rf, sm + L_FB + (q0 + q) * 16384 + i * 1024, voff, sbase + (q0 + q) * 128 + i * 8 * (DM * 2));
    u32x4 bfr[4];
#pragma unroll
    for (int ks = 0; ks < 4; ++ks) bfr[ks] = *(LAS u32x4*)(sm + L_HS + (lane & 7) * ACT_PITCH + (32 * ks + 8 * g4) * 2);
    f32x4 acc[2][4];
#pragma unroll
    for (int q = 0; q < 2; ++q) {
      if (q == 0) WAIT_VM(16); else WAIT_VM(0);
#pragma unroll
      for (int t = 0; t < 4; ++t) {
        acc[q][t] = f32x4{0.f, 0.f, 0.f, 0.f};
#pragma unroll
        for (int ks = 0; ks < 4; ++ks) acc[q][t] = mfma16(afrag_lin<128>(sm + L_FB + (q0 + q) * 16384, 32 * ks, t, lane), bfr[ks], acc[q][t]);
      }
    }
#pragma unroll
    for (int q = 0; q < 2; ++q)
#pragma unroll
      for (int t = 0; t < 4; ++t)
        stg_frag(rws, W_BP + (((f2 * 8 + (lane & 7)) * 512) + f2c * 128 + 32 * (q0 + q) + 8 * t + 2 * g4) * 16, acc[q][t], n16, tag);
  };
  if (wave < NSW) {
    tag = __builtin_amdgcn_readfirstlane(*(LAS u32*)(sm + L_FLG));
    lds_wait(sm + L_FLG + 52, role2 ? (u32)NSV : 1u);
    s16x8 wa[2][4];
#pragma unroll
    for (int c = 0; c < 2; ++c)
#pragma unroll
      for (int ks = 0; ks < 4; ++ks) wa[c][ks] = afrag_swz(sm + L_WO, 32 * ks, wave + 4 * c, lane);
    SCHED();
    lds_wait(sm + L_FLG + 48, 1u);
    f32x4 acc[2] = {{0.f, 0.f, 0.f, 0.f}, {0.f, 0.f, 0.f, 0.f}};
#pragma unroll
    for (int ks = 0; ks < 4; ++ks) {
      const u32x4 b = *(LAS u32x4*)(sm + L_OS + (lane & 7) * ACT_PITCH + (32 * ks + 8 * g4) * 2);
      acc[0] = mfma16(wa[0][ks], b, acc[0]);
      acc[1] = mfma16(wa[1][ks], b, acc[1]);
    }
#pragma unroll
    for (int c = 0; c < 2; ++c)
      stg_frag(rws, W_AP + ((((bb * 32 + hh) * 8 + (lane & 7)) * 64) + 8 * (wave + 4 * c) + 2 * g4) * 16, acc[c], n16, tag);
  } else if (wave == NSW + 2) {
    for (u32 spin = 0; spin < 65536u; ++spin) {
      if (__builtin_amdgcn_readfirstlane(lds_ld(sm + L_FLG + 32)) >= (u32)(TPW - 16)) break;
      __builtin_amdgcn_s_sleep(16);
    }
    for (u32 spin = 0; spin < SPIN_CAP; ++spin) {
      if (__builtin_amdgcn_readfirstlane(lds_ld(sm + L_FLG + 28)) >= (u32)WOTRIG) break;
      __builtin_amdgcn_s_sleep(2);
    }
    if (role2) {
      dn_pair(0);
    } else {
      const rsrc_t ro = mk_rsrc(p.wo, NH * DH * DM * 2);
#pragma unroll
      for (int i = 0; i < 32; ++i) {
        const u32 row = 4 * i + g4;
        dma16(ro, sm + L_WO + i * 1024, row * 2048 + ((n16 ^ swz(row)) << 4), hh * 128 * 2048 + bb * 256);
      }
      WAIT_VM(0);
      lds_arrive(sm + L_FLG + 52, lane);
    }
  } else if (wave == NSW + 3) {
    if (role2) dn_pair(2);
  } else if (wave == NSW + 1) {
    for (u32 spin = 0; spin < 65536u; ++spin) {
      if (__builtin_amdgcn_readfirstlane(lds_ld(sm + L_FLG + 32)) >= (u32)(TPW - 16)) break;
      __builtin_amdgcn_s_sleep(16);
    }
    lds_wait(sm + L_FLG + 28, NSW);
    const int half = lane >> 5;
    f32x4 mv[2], lv[2], av[8];
#pragma unroll
    for (int w = 0; w < 2; ++w) { mv[w] = *(LAS f32x4*)(sm + L_ML + half * 32 + w * 16); lv[w] = *(LAS f32x4*)(sm + L_ML + 64 + half * 32 + w * 16); }
#pragma unroll
    for (int r = 0; r < 8; ++r) av[r] = *(LAS f32x4*)(sm + L_MRG + (half * 8 + r) * 512 + (lane & 31) * 16);
    float M = -INFINITY;
#pragma unroll
    for (int r = 0; r < 8; ++r) M = fmaxf(M, mv[r >> 2][r & 3]);
    M = max32(M);
    float L = 0.f; f32x4 o = {0.f, 0.f, 0.f, 0.f};
#pragma unroll
    for (int r = 0; r < 8; ++r) {
      const float e = __builtin_amdgcn_exp2f(mv[r >> 2][r & 3] - M);
      L = fmaf(e, lv[r >> 2][r & 3], L);
      o += av[r] * e;
    }
    L = sum32(L);
#pragma unroll
    for (int i = 0; i < 4; ++i) o[i] = sum32(o[i]);
    const float inv = 1.f / L;
    if (lane < 32) stg_sc1(rws, W_OG + ((hh * 8 + bb) * 32 + lane) * 16, pk2(o[0] * inv, o[1] * inv), pk2(o[2] * inv, o[3] * inv), tag);
    __builtin_amdgcn_s_sleep(OGDELAY);
    u32x4 og[4];
    const u32 ooff = W_OG + ((hh * 8 + (lane >> 5)) * 32 + (lane & 31)) * 16;
    auto oo = [&](int i) { return ooff + i * (2 * 32 * 16); };
    gather_issue(og, rws, oo);
    gather_finish<OGSLEEP>(og, rws, oo, tag);
#pragma unroll
    for (int i = 0; i < 4; ++i)
      *(LAS u32x2*)(sm + L_OS + (2 * i + (lane >> 5)) * ACT_PITCH + (lane & 31) * 8) = u32x2{og[i][0], og[i][2]};
    lds_arrive(sm + L_FLG + 48, lane);
  } else if (wave == NSW) {
    for (u32 spin = 0; spin < 65536u; ++spin) {
      if (__builtin_amdgcn_readfirstlane(lds_ld(sm + L_FLG + 32)) >= (u32)(TPW - 16)) break;
      __builtin_amdgcn_s_sleep(16);
    }
    lds_wait(sm + L_FLG + 48, 1u);
    u32x4 ap[8];
    const u32 aoff = W_AP + (((bb * 32 + g4) * 8 + ob) * 64 + (hh & 3) * 16 + n16) * 16;
    auto ao = [&](int i) { return aoff + i * (4 * 8 * 64 * 16); };
    __builtin_amdgcn_s_sleep(APDELAY);
    u32x4 bp[7];
    const u32 boff = W_BP + ((g4 * 8 + ob) * 512 + oc / 2 + n16) * 16;
    auto bo = [&](int i) { return boff + i * (4 * 8 * 512 * 16); };
    gather_issue(ap, rws, ao);
    gather_issue(bp, rws, bo);
    gather_finish<APSLEEP>(ap, rws, ao, tag);
    gather_finish<APSLEEP>(bp, rws, bo, tag);
    float b0 = 0.f, b1 = 0.f;
#pragma unroll
    for (int i = 0; i < 7; ++i) { b0 += __uint_as_float(bp[i][0]); b1 += __uint_as_float(bp[i][2]); }
    b0 = sum32(sum16(b0)); b1 = sum32(sum16(b1));
    const float og = rbf(1.f - gate);
    b0 = rbf(og * rbf(b0)); b1 = rbf(og * rbf(b1));
    float a0 = 0.f, a1 = 0.f;
#pragma unroll
    for (int i = 0; i < 8; ++i) { a0 += __uint_as_float(ap[i][0]); a1 += __uint_as_float(ap[i][2]); }
    a0 = sum32(sum16(a0)); a1 = sum32(sum16(a1));
    const float o0 = rbf(gate * rbf(a0)) + b0, o1 = rbf(gate * rbf(a1)) + b1;
    if (lane < 16) *(u32*)(p.out + ob * DM + oc + 2 * lane) = pk2(o0, o1);
    if (j == 0 && lane == 0) __hip_atomic_store((GAS u32*)(p.ws + W_E), tag, __ATOMIC_RELAXED, __HIP_MEMORY_SCOPE_AGENT);
  }
}

extern "C" void kernel_launch(void* const* d_in, const int* in_sizes, int n_in, void* d_out, int out_size, void* d_ws,
                              size_t ws_size, hipStream_t stream) {
  Params p;
  p.x = (const u16*)d_in[0];
  p.kc = (const u16*)d_in[1];
  p.vc = (const u16*)d_in[2];
  p.wq = (const u16*)d_in[3];
  p.wo = (const u16*)d_in[4];
  p.wg = (const u16*)d_in[6];
  p.wu = (const u16*)d_in[7];
  p.wd = (const u16*)d_in[8];
  p.gw = (const u16*)d_in[9];
  p.out = (u16*)d_out;
  p.ws = (char*)d_ws;
  hipFuncSetAttribute((const void*)fused_kernel, hipFuncAttributeMaxDynamicSharedMemorySize, L_END);
  fused_kernel<<<256, NTHR, L_END, stream>>>(p);
}
```
